```python
import torch

def _softmax(x, axis=-1):
    return torch.softmax(x, dim=axis)
def _gelu(x):
    return torch.nn.functional.gelu(x, approximate="tanh")
def _silu(x):
    return torch.nn.functional.silu(x)
def _sigmoid(x):
    return torch.sigmoid(x)
def _layernorm(x, axis=-1, eps=1e-5):
    m = x.mean(dim=axis, keepdim=True)
    v = x.var(dim=axis, keepdim=True, unbiased=False)
    return (x - m) / torch.sqrt(v + eps)
def _scatter_add(tgt, idx, src, axis=0):
    return tgt.clone().index_add_(axis, idx, src)
def _segment_sum(src, seg, n):
    out = torch.zeros((n,) + src.shape[1:], dtype=src.dtype)
    return out.index_add_(0, seg, src)
def _pad(x, pw):
    flat = tuple(v for pair in reversed(pw) for v in pair)
    return torch.nn.functional.pad(x, flat)

kernel_name = 'concat_heads__linear_kernel_hedgehog__sliding_block__prompt'

def setup_inputs():

    _w = (lambda a: (lambda t: t.to(torch.bfloat16) if t.is_floating_point() else t)(torch.as_tensor(a)))
    def _A_setup():
        B, S, D, H = 1, 2048, 512, 32
        Dh = D // H
        return {
            "x": _w((lambda *s: torch.randn(*s, dtype=torch.bfloat16) * 0.1)(B, S, D)),
            "wqkv": _w((lambda *s: torch.randn(*s, dtype=torch.bfloat16) * 1.6)(D, 3 * D)),
            "w_phi": _w((lambda *s: torch.randn(*s, dtype=torch.bfloat16) * 1.6)(H, Dh, Dh) * (Dh ** -0.5)),
            "wo": _w((lambda *s: torch.randn(*s, dtype=torch.bfloat16) * 3.2)(D, D)),
        }
    _A_in = _A_setup()

    def _B_setup():
        B, S, D = 1, 2048, 512
        return {
            "x": _w((lambda *s: torch.randn(*s, dtype=torch.bfloat16) * 0.2)(B, S, D)),
            "wqkv": _w((lambda *s: torch.randn(*s, dtype=torch.bfloat16) * 0.4)(D, 3 * D)),
            "wo": _w((lambda *s: torch.randn(*s, dtype=torch.bfloat16) * 0.4)(D, D)),
        }
    _B_in = _B_setup()

    return {
        "A_x": _A_in["x"],
        "A_wqkv": _A_in["wqkv"],
        "A_w_phi": _A_in["w_phi"],
        "A_wo": _A_in["wo"],
        "B_x": _B_in["x"],
        "B_wqkv": _B_in["wqkv"],
        "B_wo": _B_in["wo"],
    }

def reference(A_x, A_wqkv, A_w_phi, A_wo, B_x, B_wqkv, B_wo):
    def _A_ref(x, wqkv, w_phi, wo):
        B, S, D = x.shape
        H = 32; Dh = D // H
        qkv = (lambda x, *d: x.permute(*d))((x @ wqkv).reshape(B, S, 3, H, Dh), 2, 0, 3, 1, 4)
        q, k, v = qkv[0], qkv[1], qkv[2]
        pq = torch.einsum("bhsd,hdf->bhsf", q, w_phi)
        pk = torch.einsum("bhsd,hdf->bhsf", k, w_phi)
        phi_q = torch.concatenate([_softmax(pq, axis=-1), _softmax(-pq, axis=-1)], axis=-1)
        phi_k = torch.concatenate([_softmax(pk, axis=-1), _softmax(-pk, axis=-1)], axis=-1)
        kv = torch.einsum("bhsf,bhsd->bhfd", phi_k, v)
        z  = torch.einsum("bhsf->bhf", phi_k)
        num = torch.einsum("bhqf,bhfd->bhqd", phi_q, kv)
        den = torch.einsum("bhqf,bhf->bhq", phi_q, z)[..., None] + 1e-6
        o = num / den
        return (lambda x, *d: x.permute(*d))(o, 0, 2, 1, 3).reshape(B, S, D) @ wo

    def _B_ref(x, wqkv, wo):
        B, S, D = x.shape
        H = 32; Dh = D // H; W = 256
        qkv = (lambda x, *d: x.permute(*d))((x @ wqkv).reshape(B, S, 3, H, Dh), 2, 0, 3, 1, 4)
        q, k, v = qkv[0], qkv[1], qkv[2]
        scores = torch.einsum("bhqd,bhkd->bhqk", q, k) * (Dh ** -0.5)
        idx = torch.arange(S)
        mask = (idx[:, None] - idx[None, :] >= 0) & (idx[:, None] - idx[None, :] < W)
        scores = torch.where(mask[None, None], scores, -1e9)
        p = _softmax(scores, axis=-1)
        o = torch.einsum("bhqk,bhkd->bhqd", p, v)
        return (lambda x, *d: x.permute(*d))(o, 0, 2, 1, 3).reshape(B, S, D) @ wo

    a = _A_ref(A_x, A_wqkv, A_w_phi, A_wo)
    b = _B_ref(B_x, B_wqkv, B_wo)
    return torch.concatenate([a, b], axis=-1)
```

```cpp
#include <hip/hip_runtime.h>
#include <hip/hip_bf16.h>
#include <stdint.h>

typedef __bf16 bf16x8 __attribute__((ext_vector_type(8)));
typedef __bf16 bf16x2 __attribute__((ext_vector_type(2)));
typedef short s16x4 __attribute__((ext_vector_type(4)));
typedef float f32x16 __attribute__((ext_vector_type(16)));
typedef float f32x4 __attribute__((ext_vector_type(4)));
typedef float f32x2 __attribute__((ext_vector_type(2)));
typedef unsigned u32x2 __attribute__((ext_vector_type(2)));
typedef unsigned u32x4 __attribute__((ext_vector_type(4)));
typedef unsigned short u16;

#define LAS __attribute__((address_space(3)))
#define DEV __device__ __forceinline__

constexpr int S = 2048, D = 512, H = 32;
constexpr float LOG2E = 1.4426950408889634f;

constexpr size_t WS_QB = 0;
constexpr size_t WS_KB = 2u << 20;
constexpr size_t WS_VB = 4u << 20;
constexpr size_t WS_QA = 6u << 20;
constexpr size_t WS_OA = 8u << 20;
constexpr size_t WS_OB = 10u << 20;
constexpr size_t WS_KVP = 12u << 20;
constexpr int KVP_FLOATS = 17 * 32;

struct Params {
  const u16* xa; const u16* wqkva; const u16* wphi; const u16* woa;
  const u16* xb; const u16* wqkvb; const u16* wob;
  u16* out; char* ws;
};

DEV unsigned pk2(float a, float b) {
  f32x2 v = {a, b};
  return __builtin_bit_cast(unsigned, __builtin_convertvector(v, bf16x2));
}
DEV f32x16 mfma32(bf16x8 a, bf16x8 b, f32x16 c) { return __builtin_amdgcn_mfma_f32_32x32x16_bf16(a, b, c, 0, 0, 0); }
DEV void glds16(const void* sbase, unsigned voff, unsigned lds) {
  unsigned keep;
  asm volatile("s_mov_b32 %0, m0\n\ts_mov_b32 m0, %3\n\ts_nop 0\n\tglobal_load_lds_dwordx4 %1, %2\n\ts_mov_b32 m0, %0"
               : "=&s"(keep) : "v"(voff), "s"(sbase), "s"(lds) : "memory");
}
DEV unsigned lds_addr(char* p) { return (unsigned)(uintptr_t)(LAS char*)p; }
DEV bf16x8 lds_b128(const char* p) { return *(const LAS bf16x8*)(p); }
DEV s16x4 lds_tr(const char* p) { return __builtin_amdgcn_ds_read_tr16_b64_v4i16((LAS s16x4*)p); }
DEV bf16x8 cat8(s16x4 a, s16x4 b) {
  u32x2 x = __builtin_bit_cast(u32x2, a), y = __builtin_bit_cast(u32x2, b);
  u32x4 r = {x.x, x.y, y.x, y.y};
  return __builtin_bit_cast(bf16x8, r);
}
DEV f32x2 xhalf(float a) {
  const unsigned u = __float_as_uint(a), v = u;
  u32x2 r = __builtin_amdgcn_permlane32_swap(u, v, false, false);
  const unsigned x = r[0], y = r[1];
  return f32x2{__uint_as_float(x), __uint_as_float(y)};
}
DEV float xhalf_max(float a) { f32x2 r = xhalf(a); return fmaxf(r.x, r.y); }
DEV float xhalf_min(float a) { f32x2 r = xhalf(a); return fminf(r.x, r.y); }
DEV float xhalf_sum(float a) { f32x2 r = xhalf(a); return r.x + r.y; }
DEV u32x4 pack_head(float r0, float r1, float r2, float r3, float r4, float r5, float r6, float r7) {
  unsigned x0 = pk2(r0, r1), x1 = pk2(r2, r3), y0 = pk2(r4, r5), y1 = pk2(r6, r7);
  u32x2 a = __builtin_amdgcn_permlane32_swap(x0, y0, false, false);
  u32x2 b = __builtin_amdgcn_permlane32_swap(x1, y1, false, false);
  u32x4 o = {a.x, b.x, a.y, b.y};
  return o;
}
#define WAIT_VM(n) asm volatile("s_waitcnt vmcnt(" #n ")" ::: "memory")
#define WAIT_LGKM0() asm volatile("s_waitcnt lgkmcnt(0)" ::: "memory")
#define BAR() __builtin_amdgcn_s_barrier()

DEV void hedgehog(bf16x8 wf, bf16x8 xf, unsigned (&p0)[4], unsigned (&p1)[4]) {
  f32x16 z = {};
  f32x16 pk = mfma32(wf, xf, z);
  float mx = fmaxf(fmaxf(fmaxf(pk[0], pk[1]), fmaxf(pk[2], pk[3])), fmaxf(fmaxf(pk[4], pk[5]), fmaxf(pk[6], pk[7])));
  float mn = fminf(fminf(fminf(pk[0], pk[1]), fminf(pk[2], pk[3])), fminf(fminf(pk[4], pk[5]), fminf(pk[6], pk[7])));
  mx = xhalf_max(mx); mn = xhalf_min(mn);
  float ep[8], en[8], sp = 0.f, sn = 0.f;
  const float a = -mx * LOG2E, b = mn * LOG2E;
#pragma unroll
  for (int i = 0; i < 8; ++i) {
    ep[i] = __builtin_amdgcn_exp2f(__builtin_fmaf(pk[i], LOG2E, a));
    en[i] = __builtin_amdgcn_exp2f(__builtin_fmaf(pk[i], -LOG2E, b));
    sp += ep[i]; sn += en[i];
  }
  sp = xhalf_sum(sp); sn = xhalf_sum(sn);
  const float rp = __builtin_amdgcn_rcpf(sp), rn = __builtin_amdgcn_rcpf(sn);
#pragma unroll
  for (int i = 0; i < 4; ++i) {
    p0[i] = pk2(ep[2 * i] * rp, ep[2 * i + 1] * rp);
    p1[i] = pk2(en[2 * i] * rn, en[2 * i + 1] * rn);
  }
}

constexpr int P1_STAGE = 40960, P1_XB = 16384;
constexpr int P1_LDS = 3 * P1_STAGE;

DEV void phase1(const Params& p, char* smem, int b) {
  const int tid = threadIdx.x, lane = tid & 63, w = __builtin_amdgcn_readfirstlane(tid >> 6);
  const int r = lane & 31, h = lane >> 5;
  const int xcd = b & 7, j = b >> 3;
  const int branch = xcd >> 2;
  const int rt = (xcd & 1) * 8 + (j & 7), hg = ((xcd >> 1) & 1) * 4 + (j >> 3);
  const int s0 = rt * 128;
  const u16* x = branch ? p.xb : p.xa;
  const u16* W = branch ? p.wqkvb : p.wqkva;
  const int rg = w >> 1, cg = w & 1;

  unsigned xs[2];
#pragma unroll
  for (int i = 0; i < 2; ++i) {
    int row = 8 * (2 * w + i) + (lane >> 3);
    xs[i] = row * 1024 + (((lane & 7) ^ ((row >> 1) & 7)) << 4);
  }
  const char* xbase = (const char*)x + (size_t)s0 * 1024;
  const int krow = 8 * w + (lane >> 3);
  const unsigned wsrc = krow * 3072 + (((lane & 7) ^ (((krow >> 1) & 1) << 2)) << 4);
  const char* wbase = (const char*)W + hg * 128;
  const unsigned lbase = lds_addr(smem);
  auto stage = [&](int kt, int buf) {
    const unsigned base = lbase + buf * P1_STAGE;
#pragma unroll
    for (int i = 0; i < 2; ++i) glds16(xbase + kt * 128, xs[i], base + (2 * w + i) * 1024);
#pragma unroll
    for (int t = 0; t < 3; ++t) glds16(wbase + (size_t)kt * 64 * 3072 + t * 1024, wsrc, base + P1_XB + t * 8192 + w * 1024);
  };
  stage(0, 0); stage(1, 1); stage(2, 2);

  bf16x8 wf[2];
  auto load_wf = [&]() {
#pragma unroll
    for (int hh = 0; hh < 2; ++hh) {
      const u16* wp = p.wphi + (size_t)(hg * 4 + cg * 2 + hh) * 256 + (r & 15);
      u16 v[8];
#pragma unroll
      for (int e = 0; e < 8; ++e) v[e] = wp[(8 * (e >> 2) + 4 * h + (e & 3)) * 16];
      u32x4 u = {v[0] | ((unsigned)v[1] << 16), v[2] | ((unsigned)v[3] << 16), v[4] | ((unsigned)v[5] << 16), v[6] | ((unsigned)v[7] << 16)};
      wf[hh] = __builtin_bit_cast(bf16x8, u);
    }
  };

  int xoff[4];
  {
    const int row = 32 * rg + r;
#pragma unroll
    for (int ks = 0; ks < 4; ++ks) xoff[ks] = row * 128 + (((2 * ks + h) ^ ((row >> 1) & 7)) << 4);
  }
  const int q4 = (lane & 15) >> 2, p4 = lane & 3, blk = (lane >> 4) & 1;
  const int woff = P1_XB + (8 * h + q4) * 128 + (((4 * cg + 2 * blk + (p4 >> 1)) ^ ((q4 >> 1) << 2)) << 4) + 8 * (p4 & 1);

  f32x16 acc[3] = {};
#pragma unroll
  for (int kt = 0; kt < 8; ++kt) {
    if (kt == 0) WAIT_VM(10); else if (kt < 7) WAIT_VM(5); else WAIT_VM(0);
    WAIT_LGKM0();
    BAR();
    if (kt >= 1 && kt + 2 < 8) stage(kt + 2, (kt + 2) % 3);
    if (kt == 7 && !branch) load_wf();
    const char* base = smem + (kt % 3) * P1_STAGE;
#pragma unroll
    for (int ks = 0; ks < 4; ++ks) {
      bf16x8 xf = lds_b128(base + xoff[ks]);
#pragma unroll
      for (int t = 0; t < 3; ++t) {
        s16x4 a0 = lds_tr(base + woff + t * 8192 + ks * 2048);
        s16x4 a1 = lds_tr(base + woff + t * 8192 + ks * 2048 + 512);
        acc[t] = mfma32(cat8(a0, a1), xf, acc[t]);
      }
    }
  }

  const int srow = s0 + 32 * rg + r;
  if (branch) {
    const float qs = 0.25f * LOG2E;
#pragma unroll
    for (int t = 0; t < 3; ++t) {
      char* dst = p.ws + (t == 0 ? WS_QB : t == 1 ? WS_KB : WS_VB);
#pragma unroll
      for (int hh = 0; hh < 2; ++hh) {
        const float sc = t == 0 ? qs : 1.f;
        u32x4 o = pack_head(acc[t][8 * hh + 0] * sc, acc[t][8 * hh + 1] * sc, acc[t][8 * hh + 2] * sc, acc[t][8 * hh + 3] * sc,
                            acc[t][8 * hh + 4] * sc, acc[t][8 * hh + 5] * sc, acc[t][8 * hh + 6] * sc, acc[t][8 * hh + 7] * sc);
        const int head = hg * 4 + cg * 2 + hh;
        *(u32x4*)(dst + ((size_t)head * S + srow) * 32 + h * 16) = o;
      }
    }
  } else {
#pragma unroll
    for (int hh = 0; hh < 2; ++hh) {
      u32x4 o = pack_head(acc[0][8 * hh + 0], acc[0][8 * hh + 1], acc[0][8 * hh + 2], acc[0][8 * hh + 3],
                          acc[0][8 * hh + 4], acc[0][8 * hh + 5], acc[0][8 * hh + 6], acc[0][8 * hh + 7]);
      const int head = hg * 4 + cg * 2 + hh;
      *(u32x4*)(p.ws + WS_QA + ((size_t)head * S + srow) * 32 + h * 16) = o;
    }
    WAIT_LGKM0();
    BAR();
    char* Pimg = smem; char* Vimg = smem + 32768;
    const int sl = 32 * rg + r, swz = (sl >> 1) & 7;
#pragma unroll
    for (int hh = 0; hh < 2; ++hh) {
      u32x4 kb = {pk2(acc[1][8 * hh + 0], acc[1][8 * hh + 1]), pk2(acc[1][8 * hh + 2], acc[1][8 * hh + 3]),
                  pk2(acc[1][8 * hh + 4], acc[1][8 * hh + 5]), pk2(acc[1][8 * hh + 6], acc[1][8 * hh + 7])};
      unsigned p0[4], p1[4];
      hedgehog(wf[hh], __builtin_bit_cast(bf16x8, kb), p0, p1);
      const int hl = cg * 2 + hh;
      char* prow = Pimg + hl * 8192 + sl * 64;
      char* vrow = Vimg + hl * 8192 + sl * 64;
      *(LAS u32x2*)(prow + (((0 + h) ^ swz) << 3)) = u32x2{p0[0], p0[1]};
      *(LAS u32x2*)(prow + (((2 + h) ^ swz) << 3)) = u32x2{p0[2], p0[3]};
      *(LAS u32x2*)(prow + (((4 + h) ^ swz) << 3)) = u32x2{p1[0], p1[1]};
      *(LAS u32x2*)(prow + (((6 + h) ^ swz) << 3)) = u32x2{p1[2], p1[3]};
      *(LAS u32x2*)(vrow + (((0 + h) ^ swz) << 3)) = u32x2{pk2(acc[2][8 * hh + 0], acc[2][8 * hh + 1]), pk2(acc[2][8 * hh + 2], acc[2][8 * hh + 3])};
      *(LAS u32x2*)(vrow + (((2 + h) ^ swz) << 3)) = u32x2{pk2(acc[2][8 * hh + 4], acc[2][8 * hh + 5]), pk2(acc[2][8 * hh + 6], acc[2][8 * hh + 7])};
      *(LAS u32x2*)(vrow + (((4 + h) ^ swz) << 3)) = u32x2{0x3F803F80u, 0x3F803F80u};
      *(LAS u32x2*)(vrow + (((6 + h) ^ swz) << 3)) = u32x2{0x3F803F80u, 0x3F803F80u};
    }
    WAIT_LGKM0();
    BAR();
    if (w < 4) {
      f32x16 kv = {};
#pragma unroll
      for (int ks = 0; ks < 8; ++ks) {
        s16x4 a[2], bb[2];
#pragma unroll
        for (int tt = 0; tt < 2; ++tt) {
          const int row = 16 * ks + 8 * h + 4 * tt + q4;
          const int o = w * 8192 + row * 64 + (((4 * blk + p4) ^ ((row >> 1) & 7)) << 3);
          a[tt] = lds_tr(Pimg + o);
          bb[tt] = lds_tr(Vimg + o);
        }
        kv = mfma32(cat8(a[0], a[1]), cat8(bb[0], bb[1]), kv);
      }
      if (r <= 16) {
        float* dst = (float*)(p.ws + WS_KVP) + ((size_t)rt * H + hg * 4 + w) * KVP_FLOATS + r * 32 + 4 * h;
#pragma unroll
        for (int g = 0; g < 4; ++g) *(f32x4*)(dst + 8 * g) = f32x4{kv[4 * g], kv[4 * g + 1], kv[4 * g + 2], kv[4 * g + 3]};
      }
    }
  }
}

constexpr int P2_K = 0, P2_V = 16384, P2_ONES = 32768, P2_KV = 32768 + 1536;
constexpr int P2_LDS = P2_KV + 17 * 64;

DEV void phase2(const Params& p, char* smem, int b) {
  const int tid = threadIdx.x, lane = tid & 63, w = __builtin_amdgcn_readfirstlane(tid >> 6);
  const int r = lane & 31, h = lane >> 5;
  const int head = b >> 3, ch = b & 7;
  const int q0 = ch * 256;
  const char* Kg = p.ws + WS_KB + (size_t)head * S * 32;
  const char* Vg = p.ws + WS_VB + (size_t)head * S * 32;
#pragma unroll
  for (int i = 0; i < 2; ++i) {
    const int piece = 2 * w + i;
    if (ch > 0 || piece >= 8) {
      const ptrdiff_t key0 = q0 - 256 + piece * 32;
      glds16(Kg + key0 * 32, (lane >> 1) * 32 + (((lane & 1) ^ ((lane >> 4) & 1)) << 4), lds_addr(smem) + P2_K + piece * 1024);
      glds16(Vg + key0 * 32, lane * 16, lds_addr(smem) + P2_V + piece * 1024);
    }
  }
  const int qrow = q0 + 32 * w + r;
  const bf16x8 qf = *(const bf16x8*)(p.ws + WS_QB + ((size_t)head * S + qrow) * 32 + h * 16);
  const bf16x8 qa = *(const bf16x8*)(p.ws + WS_QA + ((size_t)head * S + qrow) * 32 + h * 16);
  bf16x8 wf;
  {
    const u16* wp = p.wphi + (size_t)head * 256 + (r & 15);
    u16 v[8];
#pragma unroll
    for (int e = 0; e < 8; ++e) v[e] = wp[(8 * h + e) * 16];
    u32x4 u = {v[0] | ((unsigned)v[1] << 16), v[2] | ((unsigned)v[3] << 16), v[4] | ((unsigned)v[5] << 16), v[6] | ((unsigned)v[7] << 16)};
    wf = __builtin_bit_cast(bf16x8, u);
  }
  if (tid < KVP_FLOATS / 2) {
    const float* src = (const float*)(p.ws + WS_KVP) + (size_t)head * KVP_FLOATS + 2 * tid;
    f32x2 v[16];
#pragma unroll
    for (int i = 0; i < 16; ++i) v[i] = *(const f32x2*)(src + (size_t)i * H * KVP_FLOATS);
    f32x2 s = v[0];
#pragma unroll
    for (int i = 1; i < 16; ++i) s += v[i];
    *(LAS unsigned*)(smem + P2_KV + tid * 4) = pk2(s.x, s.y);
  }
  if (tid < 384) *(LAS unsigned*)(smem + P2_ONES + tid * 4) = 0x3F803F80u;
  WAIT_VM(0);
  WAIT_LGKM0();
  BAR();

  {
    unsigned p0[4], p1[4];
    hedgehog(wf, qa, p0, p1);
    const int c = r < 16 ? r : 16;
    const char* kvp = smem + P2_KV + c * 64 + 8 * h;
    u32x2 a0 = *(const LAS u32x2*)(kvp), a1 = *(const LAS u32x2*)(kvp + 16), a2 = *(const LAS u32x2*)(kvp + 32), a3 = *(const LAS u32x2*)(kvp + 48);
    f32x16 o = {};
    o = mfma32(__builtin_bit_cast(bf16x8, u32x4{a0.x, a0.y, a1.x, a1.y}), __builtin_bit_cast(bf16x8, u32x4{p0[0], p0[1], p0[2], p0[3]}), o);
    o = mfma32(__builtin_bit_cast(bf16x8, u32x4{a2.x, a2.y, a3.x, a3.y}), __builtin_bit_cast(bf16x8, u32x4{p1[0], p1[1], p1[2], p1[3]}), o);
    const float inv = 1.0f / (o[8] + 1e-6f);
    u32x4 ov = pack_head(o[0] * inv, o[1] * inv, o[2] * inv, o[3] * inv, o[4] * inv, o[5] * inv, o[6] * inv, o[7] * inv);
    *(u32x4*)(p.ws + WS_OA + ((size_t)head * S + qrow) * 32 + h * 16) = ov;
  }

  {
    const int tmin = ch == 0 ? 8 - w : 0;
    const int koff = P2_K + (32 * w + r) * 32 + ((h ^ ((r >> 3) & 1)) << 4);
    bf16x8 kf[9];
#pragma unroll
    for (int t = 0; t < 9; ++t) kf[t] = lds_b128(smem + koff + t * 1024);
    f32x16 mfar, mdiag;
#pragma unroll
    for (int i = 0; i < 16; ++i) {
      const int kl = (i & 3) + 8 * (i >> 2) + 4 * h;
      mfar[i] = kl > r ? 0.f : -1e30f;
      mdiag[i] = kl <= r ? 0.f : -1e30f;
    }
    float m = -1e30f;
    const f32x16 zero = {};
#pragma unroll
    for (int t = 0; t < 9; ++t) {
      if (t >= tmin) {
        f32x16 s = mfma32(kf[t], qf, t == 0 ? mfar : t == 8 ? mdiag : zero);
#pragma unroll
        for (int i = 0; i < 16; i += 2) m = fmaxf(m, fmaxf(s[i], s[i + 1]));
      }
    }
    m = xhalf_max(m);
    f32x16 negm;
#pragma unroll
    for (int i = 0; i < 16; ++i) { negm[i] = -m; mfar[i] -= m; mdiag[i] -= m; }
    const int q4 = (lane & 15) >> 2, p4 = lane & 3, blk = (lane >> 4) & 1;
    const int vstep = blk ? 0 : 1024;
    int voff = blk ? P2_ONES + (h ? 0 : 128) + 8 * p4 : P2_V + (32 * w + 4 * h + q4) * 32 + 8 * p4;
    f32x16 o = {};
#pragma unroll
    for (int t = 0; t < 9; ++t) {
      if (t >= tmin) {
        f32x16 s = mfma32(kf[t], qf, t == 0 ? mfar : t == 8 ? mdiag : negm);
        unsigned pp[8];
#pragma unroll
        for (int i = 0; i < 8; ++i) pp[i] = pk2(__builtin_amdgcn_exp2f(s[2 * i]), __builtin_amdgcn_exp2f(s[2 * i + 1]));
#pragma unroll
        for (int ks = 0; ks < 2; ++ks) {
          s16x4 v0 = lds_tr(smem + voff + ks * 512), v1 = lds_tr(smem + voff + ks * 512 + 256);
          o = mfma32(cat8(v0, v1), __builtin_bit_cast(bf16x8, u32x4{pp[4 * ks], pp[4 * ks + 1], pp[4 * ks + 2], pp[4 * ks + 3]}), o);
        }
      }
      voff += vstep;
    }
    const float inv = 1.0f / o[8];
    u32x4 ov = pack_head(o[0] * inv, o[1] * inv, o[2] * inv, o[3] * inv, o[4] * inv, o[5] * inv, o[6] * inv, o[7] * inv);
    *(u32x4*)(p.ws + WS_OB + ((size_t)head * S + qrow) * 32 + h * 16) = ov;
  }
}

constexpr int P3_LDS = 65536;

DEV void phase3(const Params& p, char* smem, int b) {
  const int tid = threadIdx.x, lane = tid & 63, w = __builtin_amdgcn_readfirstlane(tid >> 6);
  const int r = lane & 31, h = lane >> 5;
  const int xcd = b & 7, j = b >> 3;
  const int branch = xcd >> 2;
  const int rt = (xcd & 3) * 4 + (j & 3), ct = j >> 2;
  const u16* W = branch ? p.wob : p.woa;
  const char* O = p.ws + (branch ? WS_OB : WS_OA);
  const int rg = w & 3, kh = w >> 2;
#pragma unroll
  for (int i = 0; i < 8; ++i) {
    const int kr = lane >> 3;
    glds16((const char*)W + (size_t)(w * 8 + i) * 8192 + ct * 128, kr * 1024 + (((lane & 7) ^ (((kr >> 1) & 1) << 2)) << 4), lds_addr(smem) + (w * 8 + i) * 1024);
  }
  bf16x8 of[16];
  const char* osrc = O + ((size_t)(kh * 16) * S + rt * 128 + rg * 32 + r) * 32 + h * 16;
#pragma unroll
  for (int i = 0; i < 16; ++i) of[i] = *(const bf16x8*)(osrc + (size_t)i * S * 32);
  WAIT_VM(0);
  BAR();
  const int q4 = (lane & 15) >> 2, p4 = lane & 3, blk = (lane >> 4) & 1;
  f32x16 acc[2] = {};
#pragma unroll
  for (int c = 0; c < 2; ++c) {
    const int woff = (kh * 256 + 8 * h + q4) * 128 + (((4 * c + 2 * blk + (p4 >> 1)) ^ ((q4 >> 1) << 2)) << 4) + 8 * (p4 & 1);
#pragma unroll
    for (int i = 0; i < 16; ++i) {
      s16x4 a0 = lds_tr(smem + woff + i * 2048), a1 = lds_tr(smem + woff + i * 2048 + 512);
      acc[c] = mfma32(cat8(a0, a1), of[i], acc[c]);
    }
  }
  WAIT_LGKM0();
  BAR();
  float* red = (float*)smem;
  {
    const int give = kh ? 0 : 1;
    char* dst = smem + (rg * 2 + give) * 4096 + lane * 16;
#pragma unroll
    for (int g = 0; g < 4; ++g) {
      f32x4 v = kh ? f32x4{acc[0][4 * g], acc[0][4 * g + 1], acc[0][4 * g + 2], acc[0][4 * g + 3]}
                   : f32x4{acc[1][4 * g], acc[1][4 * g + 1], acc[1][4 * g + 2], acc[1][4 * g + 3]};
      *(LAS f32x4*)(dst + g * 1024) = v;
    }
  }
  WAIT_LGKM0();
  BAR();
  {
    const char* src = smem + (rg * 2 + kh) * 4096 + lane * 16;
    float f[16];
#pragma unroll
    for (int g = 0; g < 4; ++g) {
      f32x4 v = *(const LAS f32x4*)(src + g * 1024);
#pragma unroll
      for (int e = 0; e < 4; ++e) f[4 * g + e] = v[e] + (kh ? acc[1][4 * g + e] : acc[0][4 * g + e]);
    }
    u16* dst = p.out + (size_t)(rt * 128 + rg * 32 + r) * 1024 + branch * 512 + ct * 64 + kh * 32 + h * 8;
#pragma unroll
    for (int g = 0; g < 2; ++g) {
      u32x4 o = pack_head(f[8 * g], f[8 * g + 1], f[8 * g + 2], f[8 * g + 3], f[8 * g + 4], f[8 * g + 5], f[8 * g + 6], f[8 * g + 7]);
      *(u32x4*)(dst + g * 16) = o;
    }
  }
  (void)red;
}

__global__ void __launch_bounds__(512, 2) k_phase1(Params p) { extern __shared__ __attribute__((aligned(16))) char smem[]; phase1(p, smem, blockIdx.x); }
__global__ void __launch_bounds__(512, 2) k_phase2(Params p) { extern __shared__ __attribute__((aligned(16))) char smem[]; phase2(p, smem, blockIdx.x); }
__global__ void __launch_bounds__(512, 2) k_phase3(Params p) { extern __shared__ __attribute__((aligned(16))) char smem[]; phase3(p, smem, blockIdx.x); }

extern "C" void kernel_launch(void* const* d_in, const int* in_sizes, int n_in, void* d_out, int out_size, void* d_ws, size_t ws_size,
                              hipStream_t stream) {
  Params p;
  p.xa = (const u16*)d_in[0]; p.wqkva = (const u16*)d_in[1]; p.wphi = (const u16*)d_in[2]; p.woa = (const u16*)d_in[3];
  p.xb = (const u16*)d_in[4]; p.wqkvb = (const u16*)d_in[5]; p.wob = (const u16*)d_in[6];
  p.out = (u16*)d_out; p.ws = (char*)d_ws;
  hipFuncSetAttribute((const void*)k_phase1, hipFuncAttributeMaxDynamicSharedMemorySize, P1_LDS);
  hipFuncSetAttribute((const void*)k_phase3, hipFuncAttributeMaxDynamicSharedMemorySize, P3_LDS);
  k_phase1<<<256, 512, P1_LDS, stream>>>(p);
  k_phase2<<<256, 512, P2_LDS, stream>>>(p);
  k_phase3<<<256, 512, P3_LDS, stream>>>(p);
}
```

```cpp
#include <hip/hip_runtime.h>
#include <hip/hip_bf16.h>
#include <stdint.h>

typedef __bf16 bf16x8 __attribute__((ext_vector_type(8)));
typedef __bf16 bf16x2 __attribute__((ext_vector_type(2)));
typedef short s16x4 __attribute__((ext_vector_type(4)));
typedef float f32x16 __attribute__((ext_vector_type(16)));
typedef float f32x4 __attribute__((ext_vector_type(4)));
typedef float f32x2 __attribute__((ext_vector_type(2)));
typedef unsigned u32x2 __attribute__((ext_vector_type(2)));
typedef unsigned u32x4 __attribute__((ext_vector_type(4)));
typedef unsigned short u16;

#define LAS __attribute__((address_space(3)))
#define DEV __device__ __forceinline__

constexpr int S = 2048, D = 512, H = 32;
constexpr float LOG2E = 1.4426950408889634f;

constexpr size_t WS_PAD = 4096, WS_STRIDE = (2u << 20) + WS_PAD;
constexpr size_t WS_QB = WS_PAD;
constexpr size_t WS_KB = WS_PAD + WS_STRIDE;
constexpr size_t WS_VB = WS_PAD + 2 * WS_STRIDE;
constexpr size_t WS_QA = WS_PAD + 3 * WS_STRIDE;
constexpr size_t WS_OA = WS_PAD + 4 * WS_STRIDE;
constexpr size_t WS_OB = WS_PAD + 5 * WS_STRIDE;
constexpr size_t WS_KVP = WS_PAD + 6 * WS_STRIDE;
constexpr int KVP_FLOATS = 17 * 32;
constexpr size_t WS_FLAGS = 7 * WS_STRIDE + 2 * WS_PAD;
constexpr int FL_EPOCH = 0, FL_P1 = 8, FL_P2 = 8 + 256 * 8;
constexpr unsigned WS_DATA_BYTES = 7 * WS_STRIDE;

struct Params {
  const u16* xa; const u16* wqkva; const u16* wphi; const u16* woa;
  const u16* xb; const u16* wqkvb; const u16* wob;
  u16* out; char* ws;
};

DEV unsigned pk2(float a, float b) {
  f32x2 v = {a, b};
  return __builtin_bit_cast(unsigned, __builtin_convertvector(v, bf16x2));
}
DEV f32x16 mfma32(bf16x8 a, bf16x8 b, f32x16 c) { return __builtin_amdgcn_mfma_f32_32x32x16_bf16(a, b, c, 0, 0, 0); }
DEV void glds16(const void* sbase, unsigned voff, unsigned lds) {
  unsigned keep;
  asm volatile("s_mov_b32 %0, m0\n\ts_mov_b32 m0, %3\n\ts_nop 0\n\tglobal_load_lds_dwordx4 %1, %2\n\ts_mov_b32 m0, %0"
               : "=&s"(keep) : "v"(voff), "s"(sbase), "s"(lds) : "memory");
}
DEV void glds16_sc1(const void* sbase, unsigned voff, unsigned lds) {
  unsigned keep;
  asm volatile("s_mov_b32 %0, m0\n\ts_mov_b32 m0, %3\n\ts_nop 0\n\tglobal_load_lds_dwordx4 %1, %2 sc1\n\ts_mov_b32 m0, %0"
               : "=&s"(keep) : "v"(voff), "s"(sbase), "s"(lds) : "memory");
}
DEV unsigned lds_addr(char* p) { return (unsigned)(uintptr_t)(LAS char*)p; }
DEV bf16x8 lds_b128(const char* p) { return *(const LAS bf16x8*)(p); }
DEV s16x4 lds_tr(const char* p) { return __builtin_amdgcn_ds_read_tr16_b64_v4i16((LAS s16x4*)p); }
DEV bf16x8 cat8(s16x4 a, s16x4 b) {
  u32x2 x = __builtin_bit_cast(u32x2, a), y = __builtin_bit_cast(u32x2, b);
  u32x4 r = {x.x, x.y, y.x, y.y};
  return __builtin_bit_cast(bf16x8, r);
}
DEV f32x2 xhalf(float a) {
  const unsigned u = __float_as_uint(a), v = u;
  u32x2 r = __builtin_amdgcn_permlane32_swap(u, v, false, false);
  const unsigned x = r[0], y = r[1];
  return f32x2{__uint_as_float(x), __uint_as_float(y)};
}
DEV float fmax2(float a, float b) { return __builtin_fmaxf(a, b); }
DEV float fmin2(float a, float b) { return __builtin_fminf(a, b); }
DEV float fmax3(float a, float b, float c) { return fmax2(fmax2(a, b), c); }
DEV float fmin3(float a, float b, float c) { return fmin2(fmin2(a, b), c); }
DEV float xhalf_max(float a) { f32x2 r = xhalf(a); return fmax2(r.x, r.y); }
DEV float xhalf_min(float a) { f32x2 r = xhalf(a); return fmin2(r.x, r.y); }
DEV float xhalf_sum(float a) { f32x2 r = xhalf(a); return r.x + r.y; }
DEV u32x4 pack_head(float r0, float r1, float r2, float r3, float r4, float r5, float r6, float r7) {
  unsigned x0 = pk2(r0, r1), x1 = pk2(r2, r3), y0 = pk2(r4, r5), y1 = pk2(r6, r7);
  u32x2 a = __builtin_amdgcn_permlane32_swap(x0, y0, false, false);
  u32x2 b = __builtin_amdgcn_permlane32_swap(x1, y1, false, false);
  u32x4 o = {a.x, b.x, a.y, b.y};
  return o;
}
typedef __attribute__((address_space(1))) unsigned long long gu64;
typedef unsigned long long u64;
typedef __amdgpu_buffer_rsrc_t rsrc_t;
DEV unsigned hoff(int head, int s, int h) { return (unsigned)((head * 2 + h) * S + s) * 16; }
DEV void st_sc1(rsrc_t rs, unsigned off, u32x4 v) { __builtin_amdgcn_raw_buffer_store_b128(v, rs, off, 0, 16); }
DEV u32x4 ld_sc1(rsrc_t rs, unsigned off) { return __builtin_amdgcn_raw_buffer_load_b128(rs, off, 0, 16); }
DEV u32x2 ld_sc1_b64(rsrc_t rs, unsigned off) { return __builtin_amdgcn_raw_buffer_load_b64(rs, off, 0, 16); }
DEV void wait_flags(gu64* f, u64 tag) {
#pragma nounroll
  for (unsigned spins = 0; spins < (1u << 20); ++spins) {
    const u64 v = __hip_atomic_load(f, __ATOMIC_RELAXED, __HIP_MEMORY_SCOPE_AGENT);
    if (__all(v == tag)) break;
    __builtin_amdgcn_s_sleep(1);
  }
  __builtin_amdgcn_fence(__ATOMIC_ACQUIRE, "wavefront");
  asm volatile("" ::: "memory");
}
#define WAIT_VM(n) asm volatile("s_waitcnt vmcnt(" #n ")" ::: "memory")
#define WAIT_LGKM0() asm volatile("s_waitcnt lgkmcnt(0)" ::: "memory")
#define BAR() __builtin_amdgcn_s_barrier()

DEV void hedgehog(bf16x8 wf, bf16x8 xf, unsigned (&p0)[4], unsigned (&p1)[4]) {
  f32x16 z = {};
  f32x16 pk = mfma32(wf, xf, z);
  float mx = fmax3(fmax3(pk[0], pk[1], pk[2]), fmax3(pk[3], pk[4], pk[5]), fmax2(pk[6], pk[7]));
  float mn = fmin3(fmin3(pk[0], pk[1], pk[2]), fmin3(pk[3], pk[4], pk[5]), fmin2(pk[6], pk[7]));
  mx = xhalf_max(mx); mn = xhalf_min(mn);
  float ep[8], en[8], sp = 0.f, sn = 0.f;
  const float a = -mx * LOG2E, b = mn * LOG2E;
#pragma unroll
  for (int i = 0; i < 8; ++i) {
    ep[i] = __builtin_amdgcn_exp2f(__builtin_fmaf(pk[i], LOG2E, a));
    en[i] = __builtin_amdgcn_exp2f(__builtin_fmaf(pk[i], -LOG2E, b));
    sp += ep[i]; sn += en[i];
  }
  sp = xhalf_sum(sp); sn = xhalf_sum(sn);
  const float rp = __builtin_amdgcn_rcpf(sp), rn = __builtin_amdgcn_rcpf(sn);
#pragma unroll
  for (int i = 0; i < 4; ++i) {
    p0[i] = pk2(ep[2 * i] * rp, ep[2 * i + 1] * rp);
    p1[i] = pk2(en[2 * i] * rn, en[2 * i + 1] * rn);
  }
}

constexpr int P1_STAGE = 40960, P1_XB = 16384;
constexpr int L_WPHI1 = 81920, L_WPHI2 = 83968, L_WO = 98304, LDS_BYTES = 163840;
constexpr int P2_LOOK = 40960;
constexpr int P2_K = 0, P2_V = 16384, P2_ONES = 32768, P2_KV = 32768 + 1536;

template <bool FIRST, class F>
DEV u32x4 window_attention(const char* smem, bf16x8 qf, int w, int lane, const f32x16& mfar, const f32x16& mdiag,
                           const unsigned (&keep)[8], unsigned koff, unsigned voff, F between) {
  const int tmin = FIRST ? 8 - w : 0;
  bf16x8 kf[9];
#pragma unroll
  for (int t = 0; t < 9; ++t) kf[t] = lds_b128(smem + koff + t * 1024);
  const f32x16 zero = {};
  f32x16 sc[2];
  float ma[4] = {-1e30f, -1e30f, -1e30f, -1e30f};
  if (!FIRST) sc[0] = mfma32(kf[0], qf, mfar);
#pragma unroll
  for (int t = 0; t < 9; ++t) {
    if (t < 8 && (!FIRST || t + 1 >= tmin)) sc[(t + 1) & 1] = mfma32(kf[t + 1], qf, t + 1 == 8 ? mdiag : zero);
    __builtin_amdgcn_sched_barrier(0);
    if (!FIRST || t >= tmin) {
#pragma unroll
      for (int i = 0; i < 8; ++i) ma[i & 3] = fmax3(ma[i & 3], sc[t & 1][2 * i], sc[t & 1][2 * i + 1]);
    }
    __builtin_amdgcn_sched_barrier(0);
  }
  float m = fmax2(fmax2(ma[0], ma[1]), fmax2(ma[2], ma[3]));
  m = xhalf_max(m);
  between();
  f32x16 negm;
#pragma unroll
  for (int i = 0; i < 16; ++i) negm[i] = -m;
  const int vstep = (lane & 16) ? 0 : 1024;
  f32x16 o0 = {}, o1 = {};
  s16x4 vf[2][4];
  auto fetch = [&](int t) {
    sc[t & 1] = mfma32(kf[t], qf, negm);
#pragma unroll
    for (int i = 0; i < 4; ++i) vf[t & 1][i] = lds_tr(smem + voff + t * vstep + i * 256);
  };
  if (!FIRST) fetch(0);
#pragma unroll
  for (int t = 0; t < 9; ++t) {
    if (t < 8 && (!FIRST || t + 1 >= tmin)) fetch(t + 1);
    __builtin_amdgcn_sched_barrier(0);
    if (!FIRST || t >= tmin) {
      unsigned pp[8];
#pragma unroll
      for (int i = 0; i < 4; ++i) {
        pp[i] = pk2(__builtin_amdgcn_exp2f(sc[t & 1][2 * i]), __builtin_amdgcn_exp2f(sc[t & 1][2 * i + 1]));
        if (t == 0) pp[i] &= keep[i];
        if (t == 8) pp[i] &= ~keep[i];
      }
      o0 = mfma32(cat8(vf[t & 1][0], vf[t & 1][1]), __builtin_bit_cast(bf16x8, u32x4{pp[0], pp[1], pp[2], pp[3]}), o0);
#pragma unroll
      for (int i = 4; i < 8; ++i) {
        pp[i] = pk2(__builtin_amdgcn_exp2f(sc[t & 1][2 * i]), __builtin_amdgcn_exp2f(sc[t & 1][2 * i + 1]));
        if (t == 0) pp[i] &= keep[i];
        if (t == 8) pp[i] &= ~keep[i];
      }
      o1 = mfma32(cat8(vf[t & 1][2], vf[t & 1][3]), __builtin_bit_cast(bf16x8, u32x4{pp[4], pp[5], pp[6], pp[7]}), o1);
    }
    __builtin_amdgcn_sched_barrier(0);
  }
  f32x16 o;
#pragma unroll
  for (int i = 0; i < 9; ++i) o[i] = o0[i] + o1[i];
  const float inv = 1.0f / o[8];
  return pack_head(o[0] * inv, o[1] * inv, o[2] * inv, o[3] * inv, o[4] * inv, o[5] * inv, o[6] * inv, o[7] * inv);
}

__global__ void __launch_bounds__(512, 2) k_fused(Params p) {
  extern __shared__ __attribute__((aligned(16))) char smem[];
  const int tid = threadIdx.x, lane = tid & 63, w = __builtin_amdgcn_readfirstlane(tid >> 6);
  const int r = lane & 31, h = lane >> 5;
  const int q4 = (lane & 15) >> 2, p4 = lane & 3, blk = (lane >> 4) & 1;
  const int b = blockIdx.x, xcd = b & 7, j = b >> 3;
  const int branch1 = xcd >> 2;
  const int branch = xcd >> 2;
  const unsigned lbase = lds_addr(smem);
  gu64* flags = (gu64*)(p.ws + WS_FLAGS);
  const rsrc_t rs = __builtin_amdgcn_make_buffer_rsrc(p.ws, 0, WS_DATA_BYTES, 0x00020000);

  u64 epoch;
  asm volatile("global_load_dwordx2 %0, %1, %2 sc1" : "=v"(epoch) : "v"(0), "s"(flags) : "memory");
  u64 tag;

  const int head2 = xcd * 4 + (j >> 3), ch = j & 7;
  const int rt3 = (xcd & 3) * 4 + (j & 3), ct3 = j >> 2;

  f32x16 mfar, mdiag;
  unsigned keep[8], kvdst, wpa, koff, voff, qoff, kvsrc, klds, vlds;

  {
    const int rt = (xcd & 1) * 8 + (j & 7), hg = ((xcd >> 1) & 1) * 4 + (j >> 3);
    const int s0 = rt * 128;
    const u16* x = branch1 ? p.xb : p.xa;
    const u16* W = branch1 ? p.wqkvb : p.wqkva;
    const int rg = w >> 1, cg = w & 1;
    unsigned xs[2];
#pragma unroll
    for (int i = 0; i < 2; ++i) {
      int row = 8 * (2 * w + i) + (lane >> 3);
      xs[i] = row * 1024 + (((lane & 7) ^ ((row >> 1) & 7)) << 4);
    }
    const char* xbase = (const char*)x + (size_t)s0 * 1024;
    const int krow = 8 * w + (lane >> 3);
    const unsigned wsrc = krow * 3072 + (((lane & 7) ^ (((krow >> 1) & 1) << 2)) << 4);
    const char* wbase = (const char*)W + hg * 128;
    auto piece = [&](int q, int kt, unsigned base) {
      if (q < 2) glds16(xbase + kt * 128, xs[q], base + (2 * w + q) * 1024);
      else glds16(wbase + (size_t)kt * 64 * 3072 + (q - 2) * 1024, wsrc, base + P1_XB + (q - 2) * 8192 + w * 1024);
    };
    const char* Wo = (const char*)(branch ? p.wob : p.woa) + ct3 * 128 + (size_t)w * 8 * 8192;
    const unsigned wosrc = (lane >> 3) * 1024 + (((lane & 7) ^ (((lane >> 4) & 1) << 2)) << 4);
    auto piece_wo = [&](int i) { glds16(Wo + (size_t)i * 8192, wosrc, lbase + L_WO + (w * 8 + i) * 1024); };
    if (j < 3) {
      uint64_t pc;
      asm volatile("s_getpc_b64 %0" : "=s"(pc));
      const int pc_piece = j * 8 + w;
      if (pc_piece < 18) glds16((const char*)(pc & ~(uint64_t)255) + pc_piece * 1024, lane * 16, lbase + 122880 + w * 1024);
    }
#pragma unroll
    for (int q = 0; q < 5; ++q) piece(q, 0, lbase);
#pragma unroll
    for (int q = 0; q < 5; ++q) piece(q, 1, lbase + P1_STAGE);

    int xoff[4];
    {
      const int row = 32 * rg + r;
#pragma unroll
      for (int ks = 0; ks < 4; ++ks) xoff[ks] = row * 128 + (((2 * ks + h) ^ ((row >> 1) & 7)) << 4);
    }
    const int woff = P1_XB + (8 * h + q4) * 128 + (((4 * cg + 2 * blk + (p4 >> 1)) ^ ((q4 >> 1) << 2)) << 4) + 8 * (p4 & 1);

    f32x16 acc[3] = {};
    bf16x8 xf[2];
    s16x4 wfr[2][6];
    auto frags = [&](int slot, int buf, int ks) {
      const char* base = smem + buf;
      xf[slot] = lds_b128(base + xoff[ks]);
#pragma unroll
      for (int t = 0; t < 3; ++t) {
        wfr[slot][2 * t] = lds_tr(base + woff + t * 8192 + ks * 2048);
        wfr[slot][2 * t + 1] = lds_tr(base + woff + t * 8192 + ks * 2048 + 512);
      }
    };
    auto mmas = [&](int slot) {
#pragma unroll
      for (int t = 0; t < 3; ++t) acc[t] = mfma32(cat8(wfr[slot][2 * t], wfr[slot][2 * t + 1]), xf[slot], acc[t]);
    };
    auto kstage = [&](int kt, int cur, int nxt, int pf, int mode) {
#pragma unroll
      for (int ks = 0; ks < 4; ++ks) {
        if (ks < 3) frags((ks + 1) & 1, cur, ks + 1);
        else if (mode < 2) {
          if (mode == 0) WAIT_VM(5); else WAIT_VM(4);
          WAIT_LGKM0();
          BAR();
          frags(0, nxt, 0);
        }
        if (mode == 0) {
          if (ks == 0) { piece(0, kt + 2, lbase + pf); piece(2, kt + 2, lbase + pf); }
          if (ks == 1) piece(1, kt + 2, lbase + pf);
          if (ks == 2) { piece(3, kt + 2, lbase + pf); piece(4, kt + 2, lbase + pf); }
        } else if (mode == 1) {
          if (ks == 0) {
            if (w < 2) glds16((const char*)p.wphi + hg * 2048 + w * 1024, lane * 16, lbase + L_WPHI1 + w * 1024);
            if (w == 2) glds16((const char*)p.wphi + (head2 >> 1) * 1024, lane * 16, lbase + L_WPHI2);
            piece_wo(0);
          }
          if (ks == 1) piece_wo(1);
          if (ks == 2) { piece_wo(2); piece_wo(3); }
        } else piece_wo(4 + ks);
        __builtin_amdgcn_sched_barrier(0);
        mmas(ks & 1);
        __builtin_amdgcn_sched_barrier(0);
      }
    };
    asm volatile("s_waitcnt vmcnt(5)" : "+v"(epoch) :: "memory");
    {
      const unsigned lo = __builtin_amdgcn_readfirstlane((unsigned)epoch), hi = __builtin_amdgcn_readfirstlane((unsigned)(epoch >> 32));
      tag = (((u64)hi << 32) | lo) + 1;
    }
    BAR();
    frags(0, 0, 0);
    int cur = 0, nxt = P1_STAGE, pf = 2 * P1_STAGE;
#pragma unroll 1
    for (int kt = 0; kt < 6; ++kt) {
      kstage(kt, cur, nxt, pf, 0);
      const int tmp = cur; cur = nxt; nxt = pf; pf = tmp;
    }
    kstage(6, 0, P1_STAGE, 0, 1);
    kstage(7, P1_STAGE, 0, 0, 2);

    const int srow = s0 + 32 * rg + r;
    if (branch1) {
      const float qs = 0.25f * LOG2E;
#pragma unroll
      for (int t = 0; t < 3; ++t) {
        const unsigned dst = t == 0 ? WS_QB : t == 1 ? WS_KB : WS_VB;
#pragma unroll
        for (int hh = 0; hh < 2; ++hh) {
          const float sc = t == 0 ? qs : 1.f;
          u32x4 o = pack_head(acc[t][8 * hh + 0] * sc, acc[t][8 * hh + 1] * sc, acc[t][8 * hh + 2] * sc, acc[t][8 * hh + 3] * sc,
                              acc[t][8 * hh + 4] * sc, acc[t][8 * hh + 5] * sc, acc[t][8 * hh + 6] * sc, acc[t][8 * hh + 7] * sc);
          const int head = hg * 4 + cg * 2 + hh;
          st_sc1(rs, dst + hoff(head, srow, h), o);
        }
      }
    } else {
#pragma unroll
      for (int hh = 0; hh < 2; ++hh) {
        u32x4 o = pack_head(acc[0][8 * hh + 0], acc[0][8 * hh + 1], acc[0][8 * hh + 2], acc[0][8 * hh + 3],
                            acc[0][8 * hh + 4], acc[0][8 * hh + 5], acc[0][8 * hh + 6], acc[0][8 * hh + 7]);
        const int head = hg * 4 + cg * 2 + hh;
        st_sc1(rs, WS_QA + hoff(head, srow, h), o);
      }
      WAIT_LGKM0();
      BAR();
      char* Pimg = smem; char* Vimg = smem + 32768;
      const int sl = 32 * rg + r, swz = (sl >> 1) & 7;
#pragma unroll
      for (int hh = 0; hh < 2; ++hh) {
        const int hl = cg * 2 + hh;
        const char* wp = smem + L_WPHI1 + hl * 512 + (4 * h + q4) * 32 + 8 * p4;
        const bf16x8 wf = cat8(lds_tr(wp), lds_tr(wp + 256));
        u32x4 kb = {pk2(acc[1][8 * hh + 0], acc[1][8 * hh + 1]), pk2(acc[1][8 * hh + 2], acc[1][8 * hh + 3]),
                    pk2(acc[1][8 * hh + 4], acc[1][8 * hh + 5]), pk2(acc[1][8 * hh + 6], acc[1][8 * hh + 7])};
        unsigned p0[4], p1[4];
        hedgehog(wf, __builtin_bit_cast(bf16x8, kb), p0, p1);
        char* prow = Pimg + hl * 8192 + sl * 64;
        char* vrow = Vimg + hl * 8192 + sl * 64;
        *(LAS u32x2*)(prow + (((0 + h) ^ swz) << 3)) = u32x2{p0[0], p0[1]};
        *(LAS u32x2*)(prow + (((2 + h) ^ swz) << 3)) = u32x2{p0[2], p0[3]};
        *(LAS u32x2*)(prow + (((4 + h) ^ swz) << 3)) = u32x2{p1[0], p1[1]};
        *(LAS u32x2*)(prow + (((6 + h) ^ swz) << 3)) = u32x2{p1[2], p1[3]};
        *(LAS u32x2*)(vrow + (((0 + h) ^ swz) << 3)) = u32x2{pk2(acc[2][8 * hh + 0], acc[2][8 * hh + 1]), pk2(acc[2][8 * hh + 2], acc[2][8 * hh + 3])};
        *(LAS u32x2*)(vrow + (((2 + h) ^ swz) << 3)) = u32x2{pk2(acc[2][8 * hh + 4], acc[2][8 * hh + 5]), pk2(acc[2][8 * hh + 6], acc[2][8 * hh + 7])};
        *(LAS u32x2*)(vrow + (((4 + h) ^ swz) << 3)) = u32x2{0x3F803F80u, 0x3F803F80u};
        *(LAS u32x2*)(vrow + (((6 + h) ^ swz) << 3)) = u32x2{0x3F803F80u, 0x3F803F80u};
      }
      WAIT_LGKM0();
      BAR();
      if (w < 4) {
        __builtin_amdgcn_s_setprio(2);
        f32x16 kv0 = {}, kv1 = {};
#pragma unroll
        for (int ks = 0; ks < 8; ++ks) {
          s16x4 a[2], bb[2];
#pragma unroll
          for (int tt = 0; tt < 2; ++tt) {
            const int row = 16 * ks + 8 * h + 4 * tt + q4;
            const int o = w * 8192 + row * 64 + (((4 * blk + p4) ^ ((row >> 1) & 7)) << 3);
            a[tt] = lds_tr(Pimg + o);
            bb[tt] = lds_tr(Vimg + o);
          }
          if (ks & 1) kv1 = mfma32(cat8(a[0], a[1]), cat8(bb[0], bb[1]), kv1);
          else kv0 = mfma32(cat8(a[0], a[1]), cat8(bb[0], bb[1]), kv0);
        }
        if (r <= 16) {
          const unsigned dst = WS_KVP + ((rt * H + hg * 4 + w) * KVP_FLOATS + h * 68 + r * 4) * 4;
#pragma unroll
          for (int g = 0; g < 4; ++g)
            st_sc1(rs, dst + 544 * g, __builtin_bit_cast(u32x4, f32x4{kv0[4 * g] + kv1[4 * g], kv0[4 * g + 1] + kv1[4 * g + 1],
                                                                     kv0[4 * g + 2] + kv1[4 * g + 2], kv0[4 * g + 3] + kv1[4 * g + 3]}));
        }
        __builtin_amdgcn_s_setprio(0);
      }
    }
#pragma unroll
    for (int i = 0; i < 16; ++i) {
      const int kl = (i & 3) + 8 * (i >> 2) + 4 * h;
      mfar[i] = kl > r ? 0.f : -1e30f;
      mdiag[i] = kl <= r ? 0.f : -1e30f;
    }
#pragma unroll
    for (int i = 0; i < 8; ++i) {
      const int kl = ((2 * i) & 3) + 8 * (i >> 1) + 4 * h;
      keep[i] = (kl > r ? 0x0000FFFFu : 0u) | (kl + 1 > r ? 0xFFFF0000u : 0u);
      asm volatile("" : "+v"(keep[i]));
    }
    asm volatile("" : "+v"(mfar), "+v"(mdiag));
    {
      const int i2 = 2 * tid, g = i2 / 136, rem = i2 - g * 136, hh = rem / 68, c = (rem - hh * 68) >> 2, e = rem & 3;
      kvdst = P2_KV + c * 64 + (8 * g + 4 * hh + e) * 2;
      wpa = L_WPHI2 + (head2 & 1) * 512 + (8 * h + q4) * 32 + 8 * p4;
      koff = P2_K + (32 * w + r) * 32 + ((h ^ ((r >> 3) & 1)) << 4);
      voff = blk ? P2_ONES + (h ? 0 : 128) + 8 * p4 : P2_V + (32 * w + 4 * h + q4) * 32 + 8 * p4;
      qoff = hoff(head2, ch * 256 + 32 * w + r, h);
      const int key = 64 * w + r;
      kvsrc = hoff(head2, ch * 256 - 256 + key, h);
      klds = P2_K + key * 32 + ((h ^ ((r >> 3) & 1)) << 4);
      vlds = P2_V + key * 32 + h * 16;
      asm volatile("" : "+v"(kvdst), "+v"(wpa), "+v"(koff), "+v"(voff), "+v"(qoff), "+v"(kvsrc), "+v"(klds), "+v"(vlds));
    }
    WAIT_VM(0);
    WAIT_LGKM0();
    BAR();
    if (tid == 0) __hip_atomic_store(flags + FL_P1 + 8 * (branch1 * 128 + hg * 16 + rt), tag, __ATOMIC_RELAXED, __HIP_MEMORY_SCOPE_AGENT);
  }

  {
    const int hg = head2 >> 2;
    if (tid < 384) *(LAS unsigned*)(smem + P2_ONES + tid * 4) = 0x3F803F80u;
    {
      const int nb = ch ? 4 : 2, rtb = ch ? 2 * ch - 2 : 0;
      if (w == 0) wait_flags(flags + FL_P1 + 8 * (128 + hg * 16 + rtb + (lane < nb ? lane : 0)), tag);
      BAR();
      asm volatile("" ::: "memory");
    }
    const bf16x8 qf = __builtin_bit_cast(bf16x8, ld_sc1(rs, WS_QB + qoff));
    if (ch > 0 || w >= 4) {
      const u32x4 k0 = ld_sc1(rs, WS_KB + kvsrc), k1 = ld_sc1(rs, WS_KB + kvsrc + 512);
      const u32x4 v0 = ld_sc1(rs, WS_VB + kvsrc), v1 = ld_sc1(rs, WS_VB + kvsrc + 512);
      *(LAS u32x4*)(smem + klds) = k0;
      *(LAS u32x4*)(smem + klds + 1024) = k1;
      *(LAS u32x4*)(smem + vlds) = v0;
      *(LAS u32x4*)(smem + vlds + 1024) = v1;
    }
    asm volatile("" :: "v"(qf));
    WAIT_LGKM0();
    BAR();
    u32x4 qa_raw;
    u32x2 part[16];
    gu64* aflag = flags + FL_P1 + 8 * (hg * 16 + (lane & 15));
    glds16_sc1((const char*)(p.ws + WS_FLAGS) + (FL_P1 + 8 * hg * 16) * 8, (lane & 15) * 64, lbase + P2_LOOK + w * 1024);
    auto mid = [&]() {
      WAIT_VM(0);
      const u64 aseen = *(const LAS u64*)(smem + P2_LOOK + w * 1024 + lane * 16);
      if (!__all(aseen == tag)) wait_flags(aflag, tag);
      __builtin_amdgcn_fence(__ATOMIC_ACQUIRE, "wavefront");
      asm volatile("" ::: "memory");
      qa_raw = ld_sc1(rs, WS_QA + qoff);
      if (w < 5) {
        const unsigned src = WS_KVP + (head2 * KVP_FLOATS + 2 * (tid < KVP_FLOATS / 2 ? tid : 0)) * 4;
#pragma unroll
        for (int i = 0; i < 16; ++i) part[i] = ld_sc1_b64(rs, src + i * H * KVP_FLOATS * 4);
      }
    };
    const u32x4 ob = ch == 0 ? window_attention<true>(smem, qf, w, lane, mfar, mdiag, keep, koff, voff, mid)
                             : window_attention<false>(smem, qf, w, lane, mfar, mdiag, keep, koff, voff, mid);
    st_sc1(rs, WS_OB + qoff, ob);
    if (w < 5) {
      float sx = 0.f, sy = 0.f;
#pragma unroll
      for (int i = 0; i < 16; ++i) { const unsigned ux = part[i].x, uy = part[i].y; sx += __uint_as_float(ux); sy += __uint_as_float(uy); }
      if (tid < KVP_FLOATS / 2) *(LAS unsigned*)(smem + kvdst) = pk2(sx, sy);
    }
    const char* wp = smem + wpa;
    const bf16x8 wf = cat8(lds_tr(wp), lds_tr(wp + 128));
    unsigned p0[4], p1[4];
    hedgehog(wf, __builtin_bit_cast(bf16x8, qa_raw), p0, p1);
    WAIT_LGKM0();
    BAR();
    const int c = r < 16 ? r : 16;
    const char* kvp = smem + P2_KV + c * 64 + 8 * h;
    u32x2 a0 = *(const LAS u32x2*)(kvp), a1 = *(const LAS u32x2*)(kvp + 16), a2 = *(const LAS u32x2*)(kvp + 32), a3 = *(const LAS u32x2*)(kvp + 48);
    f32x16 o = {};
    o = mfma32(__builtin_bit_cast(bf16x8, u32x4{a0.x, a0.y, a1.x, a1.y}), __builtin_bit_cast(bf16x8, u32x4{p0[0], p0[1], p0[2], p0[3]}), o);
    o = mfma32(__builtin_bit_cast(bf16x8, u32x4{a2.x, a2.y, a3.x, a3.y}), __builtin_bit_cast(bf16x8, u32x4{p1[0], p1[1], p1[2], p1[3]}), o);
    const float inv = 1.0f / (o[8] + 1e-6f);
    u32x4 ov = pack_head(o[0] * inv, o[1] * inv, o[2] * inv, o[3] * inv, o[4] * inv, o[5] * inv, o[6] * inv, o[7] * inv);
    st_sc1(rs, WS_OA + qoff, ov);
    WAIT_VM(0);
    WAIT_LGKM0();
    BAR();
    if (tid == 0) __hip_atomic_store(flags + FL_P2 + 8 * (ch * 32 + head2), tag, __ATOMIC_RELAXED, __HIP_MEMORY_SCOPE_AGENT);
    if (b == 4 && w == 0) {
#pragma unroll
      for (int i = 0; i < 4; ++i) wait_flags(flags + FL_P1 + 8 * (64 * i + lane), tag);
      if (lane == 0) __hip_atomic_store(flags + FL_EPOCH, tag, __ATOMIC_RELAXED, __HIP_MEMORY_SCOPE_AGENT);
    }
  }

  {
    const int rg = w & 3, kh = w >> 2;
    unsigned osrc = (branch ? WS_OB : WS_OA) + hoff(kh * 16, rt3 * 128 + rg * 32 + r, h);
    unsigned wt[2];
#pragma unroll
    for (int c = 0; c < 2; ++c)
      wt[c] = L_WO + (kh * 256 + 8 * h + q4) * 128 + 8 * (p4 & 1) + (((4 * (c ^ kh) + 2 * blk + (p4 >> 1)) ^ ((q4 >> 1) << 2)) << 4);
    unsigned rgive = (rg * 2 + (kh ^ 1)) * 4096 + lane * 16, rtake = (rg * 2 + kh) * 4096 + lane * 16;
    unsigned odst = ((rt3 * 128 + rg * 32 + r) * 1024 + branch * 512 + ct3 * 64 + kh * 32 + h * 8) * 2;
    asm volatile("" : "+v"(osrc), "+v"(wt[0]), "+v"(wt[1]), "+v"(rgive), "+v"(rtake), "+v"(odst));
    if (w == 0) wait_flags(flags + FL_P2 + 8 * ((rt3 >> 1) * 32 + r), tag);
    BAR();
    asm volatile("" ::: "memory");
    u32x4 of[16];
#pragma unroll
    for (int i = 0; i < 16; ++i) of[i] = ld_sc1(rs, osrc + i * S * 32);
    __builtin_amdgcn_sched_barrier(0);
    f32x16 acc[2] = {};
#pragma unroll
    for (int i = 0; i < 16; ++i) {
#pragma unroll
      for (int c = 0; c < 2; ++c) {
        s16x4 a0 = lds_tr(smem + wt[c] + i * 2048), a1 = lds_tr(smem + wt[c] + i * 2048 + 512);
        acc[c] = mfma32(cat8(a0, a1), __builtin_bit_cast(bf16x8, of[i]), acc[c]);
      }
    }
#pragma unroll
    for (int g = 0; g < 4; ++g)
      *(LAS f32x4*)(smem + rgive + g * 1024) = f32x4{acc[1][4 * g], acc[1][4 * g + 1], acc[1][4 * g + 2], acc[1][4 * g + 3]};
    WAIT_LGKM0();
    BAR();
    float f[16];
#pragma unroll
    for (int g = 0; g < 4; ++g) {
      f32x4 v = *(const LAS f32x4*)(smem + rtake + g * 1024);
#pragma unroll
      for (int e = 0; e < 4; ++e) f[4 * g + e] = v[e] + acc[0][4 * g + e];
    }
    const rsrc_t ro = __builtin_amdgcn_make_buffer_rsrc(p.out, 0, S * 1024 * 2, 0x00020000);
#pragma unroll
    for (int g = 0; g < 2; ++g) {
      u32x4 o = pack_head(f[8 * g], f[8 * g + 1], f[8 * g + 2], f[8 * g + 3], f[8 * g + 4], f[8 * g + 5], f[8 * g + 6], f[8 * g + 7]);
      __builtin_amdgcn_raw_buffer_store_b128(o, ro, odst + g * 32, 0, 0);
    }
  }
}

extern "C" void kernel_launch(void* const* d_in, const int* in_sizes, int n_in, void* d_out, int out_size, void* d_ws, size_t ws_size,
                              hipStream_t stream) {
  Params p;
  p.xa = (const u16*)d_in[0]; p.wqkva = (const u16*)d_in[1]; p.wphi = (const u16*)d_in[2]; p.woa = (const u16*)d_in[3];
  p.xb = (const u16*)d_in[4]; p.wqkvb = (const u16*)d_in[5]; p.wob = (const u16*)d_in[6];
  p.out = (u16*)d_out; p.ws = (char*)d_ws;
  hipFuncSetAttribute((const void*)k_fused, hipFuncAttributeMaxDynamicSharedMemorySize, LDS_BYTES);
  k_fused<<<256, 512, LDS_BYTES, stream>>>(p);
}
```
